```python
import torch

def _softmax(x, axis=-1):
    return torch.softmax(x, dim=axis)
def _gelu(x):
    return torch.nn.functional.gelu(x, approximate="tanh")
def _silu(x):
    return torch.nn.functional.silu(x)
def _sigmoid(x):
    return torch.sigmoid(x)
def _layernorm(x, axis=-1, eps=1e-5):
    m = x.mean(dim=axis, keepdim=True)
    v = x.var(dim=axis, keepdim=True, unbiased=False)
    return (x - m) / torch.sqrt(v + eps)
def _scatter_add(tgt, idx, src, axis=0):
    return tgt.clone().index_add_(axis, idx, src)
def _segment_sum(src, seg, n):
    out = torch.zeros((n,) + src.shape[1:], dtype=src.dtype)
    return out.index_add_(0, seg, src)
def _pad(x, pw):
    flat = tuple(v for pair in reversed(pw) for v in pair)
    return torch.nn.functional.pad(x, flat)

kernel_name = 'parallel_sum__gqa_block_cached__glu_gated_swiglu__decode'

def setup_inputs():

    _w = (lambda a: (lambda t: t.to(torch.bfloat16) if t.is_floating_point() else t)(torch.as_tensor(a)))
    def _A_setup():
        B, Sq, D = 4, 1, 512
        Hq, Hkv, Sk, Dh = 32, 8, 4096, 128
        assert Hq % Hkv == 0, (Hq, Hkv)
        return {
            "x": _w((lambda *s: torch.randn(*s, dtype=torch.bfloat16) * 0.05)(B, Sq, D)),
            "k_cache": _w((lambda *s: torch.randn(*s, dtype=torch.bfloat16) * 1.6)(B, Hkv, Sk, Dh)),
            "v_cache": _w((lambda *s: torch.randn(*s, dtype=torch.bfloat16) * 0.8)(B, Hkv, Sk, Dh)),
            "wq": _w((lambda *s: torch.randn(*s, dtype=torch.bfloat16) * 1.6)(D, Hq * Dh) * (D ** -0.5)),
            "wo": _w((lambda *s: torch.randn(*s, dtype=torch.bfloat16) * 0.8)(Hq * Dh, D) * ((Hq * Dh) ** -0.5)),
        }
    _A_in = _A_setup()

    def _B_setup():
        B, S, D, F = 4, 1, 512, 1792
        return {
            "x": _w((lambda *s: torch.randn(*s, dtype=torch.bfloat16) * 0.05)(B, S, D)),
            "w1": _w((lambda *s: torch.randn(*s, dtype=torch.bfloat16) * 0.4)(D, F) * (2.0 / D) ** 0.5),
            "w3": _w((lambda *s: torch.randn(*s, dtype=torch.bfloat16) * 0.4)(D, F) * (2.0 / D) ** 0.5),
            "w2": _w((lambda *s: torch.randn(*s, dtype=torch.bfloat16) * 0.4)(F, D) * (2.0 / F) ** 0.5),
        }
    _B_in = _B_setup()

    return {
        "A_x": _A_in["x"],
        "A_k_cache": _A_in["k_cache"],
        "A_v_cache": _A_in["v_cache"],
        "A_wq": _A_in["wq"],
        "A_wo": _A_in["wo"],
        "B_x": _B_in["x"],
        "B_w1": _B_in["w1"],
        "B_w3": _B_in["w3"],
        "B_w2": _B_in["w2"],
    }

def reference(A_x, A_k_cache, A_v_cache, A_wq, A_wo, B_x, B_w1, B_w3, B_w2):
    def _A_ref(x, k_cache, v_cache, wq, wo):
        B, Sq, D = x.shape
        Hq, Dh = 32, 128
        Hkv = k_cache.shape[1]
        rep = Hq // Hkv
        q = (lambda x, *d: x.permute(*d))((x @ wq).reshape(B, Sq, Hq, Dh), 0, 2, 1, 3)
        k = (lambda x, n, axis: torch.repeat_interleave(x, n, dim=axis))(k_cache, rep, axis=1)
        v = (lambda x, n, axis: torch.repeat_interleave(x, n, dim=axis))(v_cache, rep, axis=1)
        scores = torch.einsum("bhqd,bhkd->bhqk", q, k) * (Dh ** -0.5)
        p = _softmax(scores, axis=-1)
        o = torch.einsum("bhqk,bhkd->bhqd", p, v)
        return (lambda x, *d: x.permute(*d))(o, 0, 2, 1, 3).reshape(B, Sq, Hq * Dh) @ wo

    def _B_ref(x, w1, w3, w2):
        gate = torch.einsum("bsd,df->bsf", x, w1)
        up = torch.einsum("bsd,df->bsf", x, w3)
        h = _silu(gate) * up
        out = torch.einsum("bsf,fd->bsd", h, w2)
        return out

    x = A_x
    xn = _layernorm(x, axis=-1)
    return x + _A_ref(xn, A_k_cache, A_v_cache, A_wq, A_wo) + _B_ref(xn, B_w1, B_w3, B_w2)
```

```cpp
#include <hip/hip_runtime.h>
#include <hip/hip_bf16.h>
#include <stdint.h>

typedef unsigned short u16;
__device__ __forceinline__ float bf2f(u16 v) { return __uint_as_float(((unsigned)v) << 16); }
__device__ __forceinline__ u16 f2bf(float f) { unsigned u = __float_as_uint(f); u += 0x7FFF + ((u >> 16) & 1); return (u16)(u >> 16); }
__device__ __forceinline__ float rbf(float f) { return f; }

constexpr int B = 4, D = 512, HQ = 32, HKV = 8, SK = 4096, DH = 128, F = 1792;
constexpr int NSPLIT = 8;

__global__ void kA(const u16* x, const u16* wq, const u16* w1, const u16* w3, float* q, float* h) {
    __shared__ float xn[B][D];
    __shared__ float red[4][B][64][2];
    int tid = threadIdx.x;
    int wv = tid >> 6, lane = tid & 63;
    {
        float v[8]; float s = 0;
        for (int i = 0; i < 8; ++i) { v[i] = bf2f(x[wv * D + lane * 8 + i]); s += v[i]; }
        for (int o = 32; o > 0; o >>= 1) s += __shfl_xor(s, o);
        float m = s / D; float ss = 0;
        for (int i = 0; i < 8; ++i) { float d = v[i] - m; ss += d * d; }
        for (int o = 32; o > 0; o >>= 1) ss += __shfl_xor(ss, o);
        float r = 1.0f / sqrtf(ss / D + 1e-5f);
        for (int i = 0; i < 8; ++i) xn[wv][lane * 8 + i] = rbf((v[i] - m) * r);
    }
    __syncthreads();
    int cb = blockIdx.x;
    bool isq = cb < 64;
    int col = (isq ? cb : cb - 64) * 64 + lane;
    float a[B] = {0, 0, 0, 0}, c[B] = {0, 0, 0, 0};
    for (int k = wv * 128; k < wv * 128 + 128; ++k) {
        if (isq) {
            float w = bf2f(wq[k * 4096 + col]);
            for (int b = 0; b < B; ++b) a[b] += xn[b][k] * w;
        } else {
            float wa = bf2f(w1[k * F + col]), wb = bf2f(w3[k * F + col]);
            for (int b = 0; b < B; ++b) { a[b] += xn[b][k] * wa; c[b] += xn[b][k] * wb; }
        }
    }
    for (int b = 0; b < B; ++b) { red[wv][b][lane][0] = a[b]; red[wv][b][lane][1] = c[b]; }
    __syncthreads();
    {
        int b = wv;
        float s0 = 0, s1 = 0;
        for (int w = 0; w < 4; ++w) { s0 += red[w][b][lane][0]; s1 += red[w][b][lane][1]; }
        if (isq) q[b * 4096 + col] = rbf(s0);
        else { float g = rbf(s0), u = rbf(s1); float sg = rbf(g / (1.0f + __expf(-g))); h[b * F + col] = rbf(sg * u); }
    }
}

__global__ void kB(const u16* kc, const u16* vc, const float* q, float* pm, float* pl, float* po) {
    __shared__ float qs[4][DH];
    __shared__ float sc[4][SK / NSPLIT];
    __shared__ float mx[4], sm[4];
    int s = blockIdx.x, kvh = blockIdx.y, b = blockIdx.z, tid = threadIdx.x;
    for (int i = tid; i < 512; i += 256) qs[i >> 7][i & 127] = q[b * 4096 + kvh * 512 + i];
    __syncthreads();
    const int NK = SK / NSPLIT;
    const u16* kb = kc + ((size_t)(b * HKV + kvh) * SK + s * NK) * DH;
    const u16* vb = vc + ((size_t)(b * HKV + kvh) * SK + s * NK) * DH;
    for (int k = tid; k < NK; k += 256) {
        float a[4] = {0, 0, 0, 0};
        for (int d = 0; d < DH; ++d) { float kv = bf2f(kb[k * DH + d]); for (int g = 0; g < 4; ++g) a[g] += qs[g][d] * kv; }
        for (int g = 0; g < 4; ++g) sc[g][k] = a[g] * 0.08838834764831845f;
    }
    __syncthreads();
    int wv = tid >> 6, lane = tid & 63;
    {
        float m = -INFINITY;
        for (int k = lane; k < NK; k += 64) m = fmaxf(m, sc[wv][k]);
        for (int o = 32; o > 0; o >>= 1) m = fmaxf(m, __shfl_xor(m, o));
        float l = 0;
        for (int k = lane; k < NK; k += 64) { float p = __expf(sc[wv][k] - m); sc[wv][k] = p; l += p; }
        for (int o = 32; o > 0; o >>= 1) l += __shfl_xor(l, o);
        if (lane == 0) { mx[wv] = m; sm[wv] = l; }
    }
    __syncthreads();
    for (int i = tid; i < 512; i += 256) {
        int g = i >> 7, d = i & 127;
        float a = 0;
        for (int k = 0; k < NK; ++k) a += sc[g][k] * bf2f(vb[k * DH + d]);
        int hh = kvh * 4 + g;
        po[((size_t)(b * HQ + hh) * NSPLIT + s) * DH + d] = a;
        if (d == 0) { pm[(b * HQ + hh) * NSPLIT + s] = mx[g]; pl[(b * HQ + hh) * NSPLIT + s] = sm[g]; }
    }
}

__global__ void kC(const float* pm, const float* pl, const float* po, float* o) {
    int bh = blockIdx.x, d = threadIdx.x;
    float M = -INFINITY;
    for (int s = 0; s < NSPLIT; ++s) M = fmaxf(M, pm[bh * NSPLIT + s]);
    float L = 0, a = 0;
    for (int s = 0; s < NSPLIT; ++s) { float w = __expf(pm[bh * NSPLIT + s] - M); L += w * pl[bh * NSPLIT + s]; a += w * po[((size_t)bh * NSPLIT + s) * DH + d]; }
    o[bh * DH + d] = rbf(a / L);
}

__global__ void kD(const u16* x, const float* o, const float* h, const u16* wo, const u16* w2, u16* out) {
    __shared__ float r1[16][64], r2[16][64];
    int b = blockIdx.y, lane = threadIdx.x & 63, wv = threadIdx.x >> 6;
    int col = blockIdx.x * 64 + lane;
    float a = 0, c = 0;
    for (int k = wv * 256; k < wv * 256 + 256; ++k) a += o[b * 4096 + k] * bf2f(wo[k * D + col]);
    for (int k = wv * 112; k < wv * 112 + 112; ++k) c += h[b * F + k] * bf2f(w2[k * D + col]);
    r1[wv][lane] = a; r2[wv][lane] = c;
    __syncthreads();
    if (wv == 0) {
        float s1 = 0, s2 = 0;
        for (int w = 0; w < 16; ++w) { s1 += r1[w][lane]; s2 += r2[w][lane]; }
        float r = rbf(bf2f(x[b * D + col]) + rbf(s1));
        out[b * D + col] = f2bf(r + rbf(s2));
    }
}

extern "C" void kernel_launch(void* const* d_in, const int* in_sizes, int n_in,
                              void* d_out, int out_size, void* d_ws, size_t ws_size,
                              hipStream_t stream) {
    const u16* x = (const u16*)d_in[0];
    const u16* kc = (const u16*)d_in[1];
    const u16* vc = (const u16*)d_in[2];
    const u16* wq = (const u16*)d_in[3];
    const u16* wo = (const u16*)d_in[4];
    const u16* w1 = (const u16*)d_in[6];
    const u16* w3 = (const u16*)d_in[7];
    const u16* w2 = (const u16*)d_in[8];
    float* ws = (float*)d_ws;
    float* q = ws;
    float* h = q + 16384;
    float* pm = h + 8192;
    float* pl = pm + 1024;
    float* po = pl + 1024;
    float* o = po + 131072;
    kA<<<64 + 28, 256, 0, stream>>>(x, wq, w1, w3, q, h);
    kB<<<dim3(NSPLIT, HKV, B), 256, 0, stream>>>(kc, vc, q, pm, pl, po);
    kC<<<128, 128, 0, stream>>>(pm, pl, po, o);
    kD<<<dim3(8, B), 1024, 0, stream>>>(x, o, h, wo, w2, (u16*)d_out);
}
```

```cpp
#include <hip/hip_runtime.h>
#include <stdint.h>

typedef unsigned v4u __attribute__((ext_vector_type(4)));
typedef unsigned v2u __attribute__((ext_vector_type(2)));
typedef float f32x4 __attribute__((ext_vector_type(4)));
typedef float f32x2 __attribute__((ext_vector_type(2)));
typedef short bf16x8 __attribute__((ext_vector_type(8)));
typedef short v4i16 __attribute__((ext_vector_type(4)));
typedef __bf16 bf16x2 __attribute__((ext_vector_type(2)));
#define LAS __attribute__((address_space(3)))
#define DI __device__ __forceinline__

constexpr int AUX_NT = 2, AUX_SC1 = 16, RSRC_W3 = 0x00020000;
constexpr unsigned SPIN_CAP = 1u << 16, LDS_SPIN_CAP = 1u << 22;
constexpr int QDELAY = 68, DLY1 = 10, DLY2 = 24;
constexpr int NEARLY = 2, VLATE = 12;

constexpr int WS_EPOCH = 0;
constexpr int WS_QBOX = 4096;
constexpr int WS_HBOX = WS_QBOX + 524288;
constexpr int WS_OBOX = WS_HBOX + 229376;
constexpr int WS_PBOX = WS_OBOX + 1048576;
constexpr int WS_BYTES = WS_PBOX + 1048576;

constexpr int PS = 1056;
constexpr int L_WO = 0, L_W2 = 16 * PS;
constexpr int L_QS = 25600, QS_RS = 320;
constexpr int L_HS = L_QS + 1280;
constexpr int L_MLM = L_HS + 128, L_MLL = L_MLM + 256;
constexpr int L_OB = L_MLL + 256;
constexpr int L_FLAG = L_OB + 256;
constexpr int L_DONE = L_FLAG + 64, L_OBF = L_DONE + 64;
enum { F_Q0, F_Q1, F_KVI, F_WO, F_KI, F_ML, F_PUB, F_H };
constexpr int L_KB = 28672;
constexpr int LDS_BYTES = L_KB + 16 * 8192;
static_assert(L_W2 + 8 * PS <= L_QS && L_OBF + 16 <= L_KB && LDS_BYTES <= 160 * 1024, "lds map");

#define TAGOK(u) ((((u).y & 0xffffu) == (tag0 & 0xffffu)) & (((u).w & 0xffffu) == (tag1 & 0xffffu)))
DI float rbf(float f) { return (float)(__bf16)f; }
DI unsigned pk(float lo, float hi) { bf16x2 v = {(__bf16)lo, (__bf16)hi}; return __builtin_bit_cast(unsigned, v); }
DI unsigned cvtpk(float lo, float hi) { unsigned r_; asm("v_cvt_pk_bf16_f32 %0, %1, %2" : "=v"(r_) : "v"(lo), "v"(hi)); return r_; }
DI float blo(unsigned u) { return __builtin_bit_cast(float, u << 16); }
DI float bhi(unsigned u) { return __builtin_bit_cast(float, u & 0xffff0000u); }
DI float u2f(unsigned u) { return __builtin_bit_cast(float, u); }
DI unsigned f2u(float f) { return __builtin_bit_cast(unsigned, f); }
DI f32x4 mfma(v4u a, v4u b, f32x4 c) {
    return __builtin_amdgcn_mfma_f32_16x16x32_bf16(__builtin_bit_cast(bf16x8, a), __builtin_bit_cast(bf16x8, b), c, 0, 0, 0);
}
DI f32x4 mfma16(v2u a, v2u b, f32x4 c) {
    return __builtin_amdgcn_mfma_f32_16x16x16bf16_1k(__builtin_bit_cast(v4i16, a), __builtin_bit_cast(v4i16, b), c, 0, 0, 0);
}
DI float dot2(unsigned a, unsigned b, float c) {
    return __builtin_amdgcn_fdot2_f32_bf16(__builtin_bit_cast(bf16x2, a), __builtin_bit_cast(bf16x2, b), c, false);
}
template <int CTRL> DI float dpp(float x) { return u2f(__builtin_amdgcn_mov_dpp(f2u(x), CTRL, 0xf, 0xf, true)); }
constexpr int XOR1 = 0xB1, XOR2 = 0x4E, ROR8 = 0x128, ROR4 = 0x124;
DI float x16_sum(float x) { auto s = __builtin_amdgcn_permlane16_swap(f2u(x), f2u(x), false, false); return u2f(s[0]) + u2f(s[1]); }
DI float x32_sum(float x) { auto s = __builtin_amdgcn_permlane32_swap(f2u(x), f2u(x), false, false); return u2f(s[0]) + u2f(s[1]); }
DI float x16_max(float x) { auto s = __builtin_amdgcn_permlane16_swap(f2u(x), f2u(x), false, false); return fmaxf(u2f(s[0]), u2f(s[1])); }
DI float x32_max(float x) { auto s = __builtin_amdgcn_permlane32_swap(f2u(x), f2u(x), false, false); return fmaxf(u2f(s[0]), u2f(s[1])); }
DI float row_sum(float x) { x += dpp<XOR1>(x); x += dpp<XOR2>(x); x += dpp<ROR4>(x); x += dpp<ROR8>(x); return x; }
DI float row_max(float x) { x = fmaxf(x, dpp<XOR1>(x)); x = fmaxf(x, dpp<XOR2>(x)); x = fmaxf(x, dpp<ROR4>(x)); x = fmaxf(x, dpp<ROR8>(x)); return x; }
DI float sum_mod4(float x) { x += dpp<ROR4>(x); x += dpp<ROR8>(x); return x32_sum(x16_sum(x)); }
DI v4u ld_nt(__amdgpu_buffer_rsrc_t r, int vo, int so) { return __builtin_amdgcn_raw_buffer_load_b128(r, vo, so, AUX_NT); }
DI v4u ld_sc1(__amdgpu_buffer_rsrc_t r, int vo, int so) { return __builtin_amdgcn_raw_buffer_load_b128(r, vo, so, AUX_SC1); }
DI void st_sc1(v4u v, __amdgpu_buffer_rsrc_t r, int vo, int so) { __builtin_amdgcn_raw_buffer_store_b128(v, r, vo, so, AUX_SC1); }
DI __amdgpu_buffer_rsrc_t mk(const void* p, unsigned bytes) { return __builtin_amdgcn_make_buffer_rsrc((void*)p, 0, bytes, RSRC_W3); }
DI v2u tr_read(LAS unsigned char* p) { return __builtin_bit_cast(v2u, __builtin_amdgcn_ds_read_tr16_b64_v4i16((LAS v4i16*)p)); }
DI v4u pack4(float a, float b, float c, float d, unsigned t0, unsigned t1) {
    const unsigned ua = f2u(a) + 0x80u, ub = f2u(b) + 0x80u, uc = f2u(c) + 0x80u, ud = f2u(d) + 0x80u;
    return v4u{__builtin_amdgcn_perm(ua, ub, 0x07060503u), __builtin_amdgcn_perm(ub, t0, 0x06050100u),
               __builtin_amdgcn_perm(uc, ud, 0x07060503u), __builtin_amdgcn_perm(ud, t1, 0x06050100u)};
}
DI f32x4 unpack4(v4u d) {
    return f32x4{u2f(d.x & 0xffffff00u), u2f(__builtin_amdgcn_perm(d.x, d.y, 0x0403020cu)),
                 u2f(d.z & 0xffffff00u), u2f(__builtin_amdgcn_perm(d.z, d.w, 0x0403020cu))};
}
#define SBAR() __builtin_amdgcn_sched_barrier(0)
#define CFENCE() asm volatile("" ::: "memory")
#define BARRIER() do { SBAR(); asm volatile("s_barrier" ::: "memory"); SBAR(); } while (0)
#define DMA1(m0v, vo, rs, so, OFF) asm volatile("s_mov_b32 m0, %0\n\ts_nop 0\n\tbuffer_load_dwordx4 %1, %2, %3 offen offset:%4 nt lds" \
    :: "s"(m0v), "v"(vo), "s"(rs), "s"(so), "i"(OFF) : "m0", "memory")
#define DMA2(m0v, va, vb, rs, so, OA, OB) asm volatile("s_mov_b32 m0, %0\n\ts_nop 4\n\tbuffer_load_dwordx4 %1, %3, %4 offen offset:%5 nt lds\n\tbuffer_load_dwordx4 %2, %3, %4 offen offset:%6 nt lds" \
    :: "s"(m0v), "v"(va), "v"(vb), "s"(rs), "s"(so), "i"(OA), "i"(OB) : "m0", "memory")
#define DMA2S(m0v, va, vb, rs, sa, sb, OA, OB) asm volatile("s_mov_b32 m0, %0\n\ts_nop 0\n\tbuffer_load_dwordx4 %1, %3, %4 offen offset:%6 nt lds\n\tbuffer_load_dwordx4 %2, %3, %5 offen offset:%7 nt lds" \
    :: "s"(m0v), "v"(va), "v"(vb), "s"(rs), "s"(sa), "s"(sb), "i"(OA), "i"(OB) : "m0", "memory")
#define LDS_INC(i) do { if (lane == 0) __hip_atomic_fetch_add((LAS unsigned*)(L + L_FLAG) + (i), 1u, __ATOMIC_RELAXED, __HIP_MEMORY_SCOPE_WORKGROUP); } while (0)
#define LDS_WAIT(i, n) do { _Pragma("nounroll") for (unsigned sp_ = 0; flag[i] < (n) && sp_ < LDS_SPIN_CAP; ++sp_) __builtin_amdgcn_s_sleep(4); CFENCE(); } while (0)
#define WAIT4(off) do { _Pragma("nounroll") for (unsigned sp_ = 0; sp_ < LDS_SPIN_CAP; ++sp_) { const v4u f_ = *(volatile LAS v4u*)(L + (off)); \
    if (__builtin_amdgcn_readfirstlane(f_.x & f_.y & f_.z & f_.w)) break; __builtin_amdgcn_s_sleep(1); } CFENCE(); } while (0)

__global__ void __launch_bounds__(1024, 4)
fused_decode(const unsigned char* __restrict__ x, const unsigned char* __restrict__ kc, const unsigned char* __restrict__ vc,
             const unsigned char* __restrict__ wq, const unsigned char* __restrict__ wo, const unsigned char* __restrict__ w1,
             const unsigned char* __restrict__ w3, const unsigned char* __restrict__ w2, unsigned char* __restrict__ out,
             unsigned char* __restrict__ wsb) {
    extern __shared__ __attribute__((aligned(16))) unsigned char lds_raw[];
    LAS unsigned char* L = (LAS unsigned char*)lds_raw;
    const int g = blockIdx.x;
    const int kvh = g & 7, j = g >> 3, b = j >> 3, s = j & 7;
    const int wave = __builtin_amdgcn_readfirstlane(threadIdx.x >> 6), lane = threadIdx.x & 63;
    const int r = lane >> 4, c = lane & 15;
    const int rbk = j >> 1, ch = j & 1, sig = kvh * 16 + rbk;
    const __amdgpu_buffer_rsrc_t rws = mk(wsb, WS_BYTES);
    volatile LAS unsigned* flag = (volatile LAS unsigned*)(L + L_FLAG);
    constexpr float LOG2E = 1.4426950408889634f;
    if (wave == 4 && lane < 36) flag[lane] = 0u;
    const unsigned lds0 = (unsigned)(uintptr_t)L;
    asm volatile("s_waitcnt lgkmcnt(0)" :: "s"(x), "s"(kc), "s"(vc), "s"(wq), "s"(wo), "s"(w1), "s"(w3), "s"(w2), "s"(out), "s"(wsb) : "memory");
#define GET_TAGS() unsigned long long ep; asm volatile("s_load_dwordx2 %0, %1, 0x0\n\ts_waitcnt lgkmcnt(0)" : "=&s"(ep) : "s"(wsb) : "memory"); \
    const unsigned tag0 = (unsigned)ep + 1u, tag1 = (unsigned)(ep >> 32) + 1u
    const size_t kvbase = (size_t)(b * 8 + kvh) << 20;
    const __amdgpu_buffer_rsrc_t rk = mk(kc + kvbase, 1 << 20), rv = mk(vc + kvbase, 1 << 20);
    const int so = (((wave + b * 8 + kvh) & 15) * 8 + ((s + wave + kvh) & 7)) * 8192;
#define K_ISSUE() do { const int v0 = r * 256 + ((c ^ (4 * r)) << 4), v1 = v0 ^ 16, v2 = v0 ^ 32, v3 = v0 ^ 48; \
        const unsigned m0a = lds0 + L_KB + wave * 8192, m0b = m0a + 4096; const int so2 = so + 4096; \
        DMA2(m0a, v0, v1, rk, so, 0, 1024); DMA2(m0a, v2, v3, rk, so, 2048, 3072); \
        DMA2(m0b, v0, v1, rk, so2, 0, 1024); DMA2(m0b, v2, v3, rk, so2, 2048, 3072); } while (0)
    if (wave >= 2) {
        if (wave >= 2 + NEARLY) BARRIER();
        K_ISSUE();
        if (wave < 2 + NEARLY) BARRIER();
    }

    if (wave < 2) {
        const __amdgpu_buffer_rsrc_t rx = mk(x, 4096);
        const int jj = (kvh & 1) ? ((j & 15) < 12 ? (j >> 4) * 12 + (j & 15) : -1) : j, tau = (kvh >> 1) * 56 + (kvh & 1) * 32 + jj, fcb = tau >> 3, fm = tau & 7;
        const bool has_tile = (wave == 0) || (jj >= 0);
        const int kq = wave == 0 ? (j & 3) : (fm & 3);
        v4u xs0, xs1, xs2, xs3, xf;
        {
            const int vs = (lane & 3) * 1024 + (lane >> 2) * 64, vf_ = (c & 3) * 1024 + r * 16 + (c >> 2) * 64, sf = kq * 256;
            asm volatile("s_nop 4\n\tbuffer_load_dwordx4 %0, %5, %7, 0 offen\n\tbuffer_load_dwordx4 %1, %5, %7, 0 offen offset:16\n\tbuffer_load_dwordx4 %2, %5, %7, 0 offen offset:32\n\t"
                         "buffer_load_dwordx4 %3, %5, %7, 0 offen offset:48\n\tbuffer_load_dwordx4 %4, %6, %7, %8 offen"
                         : "=&v"(xs0), "=&v"(xs1), "=&v"(xs2), "=&v"(xs3), "=&v"(xf) : "v"(vs), "v"(vf_), "s"(rx), "s"(sf) : "memory");
        }
        {
            const int rs = wave == 0 ? 8192 : 3584;
            const int co = has_tile ? kq * 128 * rs + (wave == 0 ? kvh * 1024 + (j >> 2) * 128 : fcb * 128) : 0;
            const unsigned nb = has_tile ? 512 * rs - co : 0;
            const __amdgpu_buffer_rsrc_t ra = mk((wave == 0 ? wq : (fm & 4) ? w3 : w1) + co, nb);
            const int ve = (lane >> 3) * rs + (((lane & 7) ^ ((lane >> 3) & 2)) << 4), vo = ve ^ 64;
            const int st = 8 * rs - 1024;
#pragma unroll
            for (int q = 0; q < 4; ++q) {
                const unsigned m = lds0 + (q < 2 ? L_KB : 0) + wave * 8192 + (q & 1) * 4096;
                const int s0_ = q * 32 * rs;
                DMA2S(m, ve, vo, ra, s0_, s0_ + st, 0, 1024); DMA2S(m, ve, vo, ra, s0_ + 2 * st, s0_ + 3 * st, 2048, 3072);
            }
        }
        SBAR();
        asm volatile("s_barrier" ::: "memory");
        GET_TAGS();
        asm volatile("s_waitcnt vmcnt(16)" : "+v"(xs0), "+v"(xs1), "+v"(xs2), "+v"(xs3), "+v"(xf) :: "memory");
        SBAR();
        v4u bx[4];
        {
            float a0 = 0.f, a1 = 0.f, q0 = 0.f, q1 = 0.f;
#pragma unroll
            for (int k = 0; k < 4; ++k) {
                a0 = dot2(xs0[k], 0x3f803f80u, a0); q0 = dot2(xs0[k], xs0[k], q0); a1 = dot2(xs1[k], 0x3f803f80u, a1); q1 = dot2(xs1[k], xs1[k], q1);
                a0 = dot2(xs2[k], 0x3f803f80u, a0); q0 = dot2(xs2[k], xs2[k], q0); a1 = dot2(xs3[k], 0x3f803f80u, a1); q1 = dot2(xs3[k], xs3[k], q1);
            }
            const float mean = sum_mod4(a0 + a1) * (1.0f / 512.0f), msq = sum_mod4(q0 + q1) * (1.0f / 512.0f);
            float var = msq - mean * mean;
            if (__builtin_expect(!__all(mean * mean <= 8.0f * var), 0)) {
                float d2 = 0.f;
#pragma unroll
                for (int k = 0; k < 4; ++k) {
                    const unsigned w4[4] = {xs0[k], xs1[k], xs2[k], xs3[k]};
#pragma unroll
                    for (int i = 0; i < 4; ++i) { const float d0 = blo(w4[i]) - mean, d1 = bhi(w4[i]) - mean; d2 = fmaf(d0, d0, d2); d2 = fmaf(d1, d1, d2); }
                }
                var = sum_mod4(d2) * (1.0f / 512.0f);
            }
            const float mb = rbf(mean), den = rbf(__builtin_amdgcn_sqrtf(rbf(rbf(var) + 1e-5f)));
            const float inv = 1.0f / den;
            v4u own;
#pragma unroll
            for (int k = 0; k < 4; ++k) { const unsigned d = cvtpk(blo(xf[k]) - mb, bhi(xf[k]) - mb); own[k] = cvtpk(blo(d) * inv, bhi(d) * inv); }
#pragma unroll
            for (int k = 0; k < 4; ++k) {
                bx[0][k] = __builtin_amdgcn_ds_swizzle(own[k], 0x13 | (0 << 5)); bx[1][k] = __builtin_amdgcn_ds_swizzle(own[k], 0x13 | (4 << 5));
                bx[2][k] = __builtin_amdgcn_ds_swizzle(own[k], 0x13 | (8 << 5)); bx[3][k] = __builtin_amdgcn_ds_swizzle(own[k], 0x13 | (12 << 5));
            }
        }
        SBAR();
        asm volatile("s_waitcnt vmcnt(0)" ::: "memory");
        LAS unsigned char* T = L + wave * 8192;
        f32x4 acc[4];
        {
            const int fl = ((c >> 3) & 1) | ((r & 1) << 1);
            LAS unsigned char* tp = T + (8 * r + (c >> 2)) * 128 + (c & 3) * 8;
#pragma unroll
            for (int ks = 0; ks < 4; ++ks)
#pragma unroll
                for (int q4 = 0; q4 < 4; ++q4) {
                    LAS unsigned char* tq = tp + ((q4 ^ fl) << 5) + (ks & 1) * 4096 + (ks < 2 ? L_KB : 0);
                    const v2u a0 = tr_read(tq), a1 = tr_read(tq + 512);
                    const v4u a = {a0.x, a0.y, a1.x, a1.y};
                    acc[q4] = mfma(a, bx[ks], ks ? acc[q4] : f32x4{0.f, 0.f, 0.f, 0.f});
                }
        }
        asm volatile("s_waitcnt lgkmcnt(0)" ::: "memory");
        {
            const int q4 = c >> 2;
            float t[4];
#pragma unroll
            for (int k = 0; k < 4; ++k) { const float lo = (q4 & 1) ? acc[1][k] : acc[0][k], hi = (q4 & 1) ? acc[3][k] : acc[2][k]; t[k] = (q4 & 2) ? hi : lo; }
            if (wave == 0) {
                int qo = WS_QBOX + (((kvh * 4 + (c & 3)) * 4 + kq) * 256 + (j >> 2) * 32 + q4 * 4 + r) * 16;
                v4u u0 = {f2u(t[0]), tag0, f2u(t[1]), tag1}, u1 = {f2u(t[2]), tag0, f2u(t[3]), tag1};
                asm volatile("" : "+v"(u0), "+v"(u1), "+v"(qo));
                st_sc1(u0, rws, qo, 0); st_sc1(u1, rws, qo + 256, 0);
            } else if (has_tile) {
                st_sc1(pack4(t[0], t[1], t[2], t[3], tag0, tag1), rws, WS_HBOX + (((fm * 28 + fcb) * 4 + q4) * 16 + (c & 3) * 4 + r) * 16, 0);
            }
        }
        if (wave == 0) flag[F_PUB] = 1u;
    }

    v4u kf[8], vf[8];
    const __amdgpu_buffer_rsrc_t rwo = mk(wo + (size_t)sig * 32 * 1024 + ch * 512, 32 * 1024 - ch * 512);
    const int rho = kvh * 14 + (rbk < 14 ? rbk : 0);
    const __amdgpu_buffer_rsrc_t rw2 = mk(w2 + (size_t)rho * 16 * 1024 + ch * 512, rbk < 14 ? 16 * 1024 - ch * 512 : 0);
#define V_ISSUE() do { _Pragma("unroll") for (int i = 0; i < 8; ++i) vf[i] = ld_nt(rv, lane * 16 + (i & 3) * 1024, so + (i >> 2) * 4096); SBAR(); LDS_INC(F_KVI); } while (0)
    if (wave < 2) K_ISSUE();
    SBAR();
    if (wave >= 2) LDS_INC(F_KI);
    GET_TAGS();
    if (wave == 2 || wave == 3) {
        const int go = WS_QBOX + ((kvh * 4 + b) * 1024 + (wave - 2) * 128 + lane) * 16;
        LDS_WAIT(F_PUB, 1u);
        __builtin_amdgcn_s_sleep(QDELAY);
        v4u qq[8];
        for (unsigned spins = 0;; ++spins) {
            CFENCE();
#pragma unroll
            for (int i = 0; i < 8; ++i) qq[i] = ld_sc1(rws, go + (i & 1) * 1024, (i >> 1) * 4096);
            SBAR();
            bool ok = true;
#pragma unroll
            for (int i = 0; i < 8; ++i) ok &= TAGOK(qq[i]);
            if (__all(ok) || spins > SPIN_CAP) break;
            __builtin_amdgcn_s_sleep(8);
        }
#pragma unroll
        for (int h = 0; h < 2; ++h) {
            const float a0 = (u2f(qq[h].x) + u2f(qq[2 + h].x)) + (u2f(qq[4 + h].x) + u2f(qq[6 + h].x));
            const float a1 = (u2f(qq[h].z) + u2f(qq[2 + h].z)) + (u2f(qq[4 + h].z) + u2f(qq[6 + h].z));
            *(LAS unsigned*)(L + L_QS + ((wave - 2) * 2 + h) * QS_RS + (lane >> 5) * 128 + c * 8 + (r & 1) * 4) = pk(a0, a1);
        }
        asm volatile("s_waitcnt lgkmcnt(0)" ::: "memory");
        flag[F_Q0 + wave - 2] = 1u;
    }
    if (wave == 13) {
        const int ho = WS_HBOX + ((((lane >> 5) * 4 + ((lane >> 4) & 1)) * 112 + rho) * 16 + c) * 16;
        v4u h0, h1;
        __builtin_amdgcn_s_setprio(3);
        for (unsigned spins = 0;; ++spins) {
            CFENCE();
            h0 = ld_sc1(rws, ho, 0); h1 = ld_sc1(rws, ho, 2 * 112 * 256);
            const bool ok = TAGOK(h0) & TAGOK(h1);
            if (__all(ok) || spins > SPIN_CAP) break;
            __builtin_amdgcn_s_sleep(8);
        }
        __builtin_amdgcn_s_setprio(0);
        const f32x4 hs = unpack4(h0) + unpack4(h1);
        float hv[4];
#pragma unroll
        for (int k = 0; k < 4; ++k) {
            const float t = rbf(x16_sum(hs[k]));
            auto sw = __builtin_amdgcn_permlane32_swap(f2u(t), f2u(t), false, false);
            const float gt = u2f(sw[0]);
            hv[k] = rbf(gt / (1.0f + __expf(-gt))) * u2f(sw[1]);
        }
        *(LAS v2u*)(L + L_HS + c * 8) = v2u{pk(hv[0], hv[1]), pk(hv[2], hv[3])};
        CFENCE(); flag[F_H] = 1u; CFENCE();
    }
    if (wave < VLATE) LDS_WAIT(F_KI, 14u);
    unsigned pt = 0;
    if (wave < 4) pt = __builtin_amdgcn_raw_buffer_load_b32(rws, WS_OBOX + ((((kvh * 16 + wave * 4 + r) * 4 + b) * 8 + s) * 16 + c) * 16, 0, AUX_SC1);
    bool vfirst = wave < VLATE;
    if (!vfirst) {
        _Pragma("nounroll") for (unsigned sp = 0; (flag[F_Q0] & flag[F_Q1]) == 0u && flag[F_KVI] < (unsigned)(VLATE - 2) && sp < LDS_SPIN_CAP; ++sp) __builtin_amdgcn_s_sleep(4);
        vfirst = __builtin_amdgcn_readfirstlane(flag[F_Q0] & flag[F_Q1]) == 0u;
    }
    if (vfirst) V_ISSUE();
    for (unsigned spins = 0; (flag[F_Q0] & flag[F_Q1]) == 0u && spins < LDS_SPIN_CAP; ++spins) __builtin_amdgcn_s_sleep(4);
    CFENCE();
    __builtin_amdgcn_s_setprio(3);
    {
        v4u qf[4];
#pragma unroll
        for (int ks = 0; ks < 4; ++ks) qf[ks] = *(LAS v4u*)(L + L_QS + (c & 3) * QS_RS + ks * 64 + r * 16);
        if (vfirst) asm volatile("s_waitcnt vmcnt(8)" ::: "memory");
        else asm volatile("s_waitcnt vmcnt(0)" ::: "memory");
        {
            LAS unsigned char* kp = L + L_KB + wave * 8192 + (4 * (c & 3) + (c >> 2)) * 256;
#pragma unroll
            for (int ks = 0; ks < 4; ++ks) { const int sl = ((4 * ks + r) ^ c) << 4; kf[ks] = *(LAS v4u*)(kp + sl); kf[4 + ks] = *(LAS v4u*)(kp + sl + 4096); }
        }
        f32x4 s0 = {0.f, 0.f, 0.f, 0.f}, s1 = {0.f, 0.f, 0.f, 0.f};
#pragma unroll
        for (int ks = 0; ks < 4; ++ks) { s0 = mfma(kf[ks], qf[ks], s0); s1 = mfma(kf[4 + ks], qf[ks], s1); }
        float sc[8];
#pragma unroll
        for (int k = 0; k < 2; ++k) {
            const unsigned a = pk(s0[2 * k], s0[2 * k + 1]), d = pk(s1[2 * k], s1[2 * k + 1]);
            const unsigned a2 = pk(blo(a) * 0.08838834764831845f, bhi(a) * 0.08838834764831845f), d2 = pk(blo(d) * 0.08838834764831845f, bhi(d) * 0.08838834764831845f);
            sc[2 * k] = blo(a2); sc[2 * k + 1] = bhi(a2); sc[4 + 2 * k] = blo(d2); sc[5 + 2 * k] = bhi(d2);
        }
        float m = sc[0];
#pragma unroll
        for (int k = 1; k < 8; ++k) m = fmaxf(m, sc[k]);
        m = x32_max(x16_max(m));
        const float ml2 = m * LOG2E;
        float p[8], l = 0.f;
#pragma unroll
        for (int k = 0; k < 8; ++k) { p[k] = __builtin_amdgcn_exp2f(fmaf(sc[k], LOG2E, -ml2)); l += p[k]; }
        l = x32_sum(x16_sum(l));
        if (lane < 4) { *(LAS float*)(L + L_MLM + (lane * 16 + wave) * 4) = ml2; *(LAS float*)(L + L_MLL + (lane * 16 + wave) * 4) = l; }
        CFENCE(); LDS_INC(F_ML); CFENCE();
        const v4u pf = {pk(p[0], p[1]), pk(p[2], p[3]), pk(p[4], p[5]), pk(p[6], p[7])};
        v4u am[4];
#pragma unroll
        for (int u = 0; u < 4; ++u) am[u] = (c >> 2) == u ? pf : v4u{0u, 0u, 0u, 0u};
        LAS unsigned char* slab = L + L_KB + wave * 8192 + c * 32 + r * 8;
        asm volatile("" : "+v"(am[0]), "+v"(am[1]), "+v"(am[2]), "+v"(am[3]), "+v"(slab));
        SBAR();
        if (!vfirst) { __builtin_amdgcn_s_setprio(0); V_ISSUE(); __builtin_amdgcn_s_setprio(3); }
        v4u bl[4], bh[4];
#pragma unroll
        for (int d = 0; d < 4; ++d) {
#pragma unroll
            for (int u = 0; u < 4; ++u) {
                bl[u][d] = __builtin_amdgcn_perm(vf[2 * d + 1][u], vf[2 * d][u], 0x05040100u);
                bh[u][d] = __builtin_amdgcn_perm(vf[2 * d + 1][u], vf[2 * d][u], 0x07060302u);
            }
            if (d < 3) SBAR();
        }
        f32x4 o0 = {0.f, 0.f, 0.f, 0.f}, o1 = {0.f, 0.f, 0.f, 0.f};
#pragma unroll
        for (int u = 0; u < 4; ++u) { o0 = mfma(am[u], bl[u], o0); o1 = mfma(am[u], bh[u], o1); }
#pragma unroll
        for (int h = 0; h < 4; ++h) *(LAS f32x2*)(slab + h * 512) = f32x2{o0[h], o1[h]};
    }
    CFENCE();
    asm volatile("" :: "v"(pt));
    flag[16 + wave] = 1u;
    CFENCE();
    __builtin_amdgcn_s_setprio(0);
    if (wave < 4) {
        const int hd = wave;
        int oofs = WS_OBOX + ((((kvh * 16 + hd * 4 + r) * 4 + b) * 8 + s) * 16 + c) * 16;
        int oo = WS_OBOX + ((sig * 4 + hd) * 128 + lane) * 16;
        LDS_WAIT(F_ML, 16u);
        const float mv = *(LAS float*)(L + L_MLM + (hd * 16 + c) * 4), lv = *(LAS float*)(L + L_MLL + (hd * 16 + c) * 4);
        const float M = row_max(mv);
        float f = __builtin_amdgcn_exp2f(mv - M);
        const float Ls = row_sum(f * lv);
        f *= __builtin_amdgcn_rcpf(Ls);
        const unsigned lse = f2u(M + __builtin_amdgcn_logf(Ls));
        unsigned ty = (lse << 16) | (tag0 & 0xffffu), tw = (lse & 0xffff0000u) | (tag1 & 0xffffu);
        float fw[16];
#pragma unroll
        for (int w = 0; w < 16; ++w) fw[w] = u2f(__builtin_amdgcn_readlane(f2u(f), w));
        LAS unsigned char* sp0 = L + L_KB + hd * 512 + lane * 8; LAS unsigned char* sp1 = sp0 + 65536;
        asm volatile("" : "+v"(oofs), "+v"(oo), "+v"(ty), "+v"(tw), "+v"(sp0), "+v"(sp1));
        __builtin_amdgcn_s_setprio(2);
        float a0 = 0.f, a1 = 0.f;
#pragma unroll
        for (int gq = 0; gq < 4; ++gq) {
            f32x2 sl[4];
            _Pragma("nounroll") for (unsigned sp = 0; sp < LDS_SPIN_CAP; ++sp) {
                const v4u f_ = *(volatile LAS v4u*)(L + L_DONE + gq * 16);
#pragma unroll
                for (int w = 0; w < 4; ++w) sl[w] = *(volatile LAS f32x2*)((gq < 2 ? sp0 : sp1) + ((4 * gq + w) & 7) * 8192);
                if (__builtin_amdgcn_readfirstlane(f_.x & f_.y & f_.z & f_.w)) break;
                __builtin_amdgcn_s_sleep(1);
            }
#pragma unroll
            for (int w = 0; w < 4; ++w) { a0 = fmaf(fw[4 * gq + w], sl[w].x, a0); a1 = fmaf(fw[4 * gq + w], sl[w].y, a1); }
            asm volatile("" : "+v"(a0), "+v"(a1));
        }
        st_sc1(v4u{f2u(a0), ty, f2u(a1), tw}, rws, oofs, 0);
        v4u d0, d1;
        __builtin_amdgcn_s_sleep(DLY1);
        for (unsigned spins = 0;; ++spins) {
            CFENCE();
            d0 = ld_sc1(rws, oo, 0); d1 = ld_sc1(rws, oo + 1024, 0);
            const bool ok = TAGOK(d0) & TAGOK(d1);
            if (__all(ok) || spins > SPIN_CAP) break;
        }
        const float e0 = u2f((d0.y >> 16) | (d0.w & 0xffff0000u)), e1 = u2f((d1.y >> 16) | (d1.w & 0xffff0000u));
        const float Mx = x32_max(x16_max(fmaxf(e0, e1)));
        const float f0 = __builtin_amdgcn_exp2f(e0 - Mx), f1 = __builtin_amdgcn_exp2f(e1 - Mx);
        const float W = x32_sum(x16_sum(f0 + f1));
        float b0 = fmaf(f0, u2f(d0.x), f1 * u2f(d1.x)), b1 = fmaf(f0, u2f(d0.z), f1 * u2f(d1.z));
        const float iw = __builtin_amdgcn_rcpf(W);
        b0 = x32_sum(x16_sum(b0)) * iw; b1 = x32_sum(x16_sum(b1)) * iw;
        *(LAS unsigned*)(L + L_OB + hd * 64 + c * 4) = pk(b0, b1);
        CFENCE(); flag[32 + hd] = 1u; CFENCE();
    } else if (wave < 12) {
        const int pw = wave - 4;
        if (pw < 4) {
            LDS_WAIT(F_KVI, 16u);
            const int to = (lane >> 5) * 1024 + (lane & 31) * 16;
#pragma unroll
            for (int i = 0; i < 4; ++i) { const unsigned m = lds0 + L_WO + (pw * 4 + i) * PS; const int sw_ = (pw * 4 + i) * 2048; DMA1(m, to, rwo, sw_, 0); }
#pragma unroll
            for (int i = 0; i < 2; ++i) { const unsigned m = lds0 + L_W2 + (pw * 2 + i) * PS; const int sw_ = (pw * 2 + i) * 2048; DMA1(m, to, rw2, sw_, 0); }
            asm volatile("s_waitcnt vmcnt(0)" ::: "memory");
            LDS_INC(F_WO);
        }
        LDS_WAIT(F_WO, 4u);
        LDS_WAIT(F_H, 1u);
        f32x4 acc[2];
        v4u wa[2];
        int pofs = WS_PBOX + (((c & 3) * 128 + sig) * 128 + ch * 64 + pw * 8 + ((c >> 2) & 1) * 4 + r) * 16;
        {
            const int q = c >> 2;
            LAS unsigned char* tp = L + L_WO + (4 * r + (q >> 1)) * PS + (q & 1) * 512 + pw * 64 + (c & 3) * 8;
            LAS unsigned char* t2 = L + L_W2 + (2 * r + (q >> 1)) * PS + (q & 1) * 512 + pw * 64 + (c & 3) * 8;
            const v2u hb = *(LAS v2u*)(L + L_HS + (c & 3) * 32 + r * 8);
#pragma unroll
            for (int k = 0; k < 2; ++k) {
                const v2u a0 = tr_read(tp + k * 32), a1 = tr_read(tp + k * 32 + 2 * PS);
                wa[k] = v4u{a0.x, a0.y, a1.x, a1.y};
                acc[k] = mfma16(tr_read(t2 + k * 32), hb, f32x4{0.f, 0.f, 0.f, 0.f});
            }
            asm volatile("" : "+v"(wa[0]), "+v"(wa[1]), "+v"(acc[0]), "+v"(acc[1]), "+v"(pofs));
        }
        __builtin_amdgcn_s_setprio(1);
        v4u ob;
        _Pragma("nounroll") for (unsigned sp = 0; sp < LDS_SPIN_CAP; ++sp) {
            const v4u f_ = *(volatile LAS v4u*)(L + L_OBF);
            ob = *(volatile LAS v4u*)(L + L_OB + (c & 3) * 64 + r * 16);
            if (__builtin_amdgcn_readfirstlane(f_.x & f_.y & f_.z & f_.w)) break;
            __builtin_amdgcn_s_sleep(1);
        }
        acc[0] = mfma(wa[0], ob, acc[0]); acc[1] = mfma(wa[1], ob, acc[1]);
        float t[4];
#pragma unroll
        for (int k = 0; k < 4; ++k) t[k] = (c & 4) ? acc[1][k] : acc[0][k];
        if (c < 8) st_sc1(pack4(t[0], t[1], t[2], t[3], tag0, tag1), rws, pofs, 0);
    } else if (wave == 12) {
        const int rb = g >> 6, rcg = (g & 7) * 8 + ((g >> 3) & 7);
        const int u = lane & 1;
        int ocol = rb * 1024 + rcg * 16 + u * 8 + r * 2;
        float xr = u2f((unsigned)*(const unsigned short*)(x + ocol) << 16);
        int po = WS_PBOX + ((rb * 128 + (lane >> 1)) * 128 + rcg * 2 + u) * 16;
        asm volatile("" : "+v"(po), "+v"(ocol), "+v"(xr));
        WAIT4(L_OBF);
        __builtin_amdgcn_s_sleep(DLY2);
        v4u d[4];
        for (unsigned spins = 0;; ++spins) {
            CFENCE();
#pragma unroll
            for (int k = 0; k < 4; ++k) d[k] = ld_sc1(rws, po, k * 32 * 2048);
            const bool ok = TAGOK(d[0]) & TAGOK(d[1]) & TAGOK(d[2]) & TAGOK(d[3]);
            if (__all(ok) || spins > SPIN_CAP) break;
        }
        const f32x4 a = (unpack4(d[0]) + unpack4(d[1])) + (unpack4(d[2]) + unpack4(d[3]));
        auto s0 = __builtin_amdgcn_permlane32_swap(f2u(a[0]), f2u(a[2]), false, false), s1 = __builtin_amdgcn_permlane32_swap(f2u(a[1]), f2u(a[3]), false, false);
        const float z0 = u2f(s0[0]) + u2f(s0[1]), z1 = u2f(s1[0]) + u2f(s1[1]);
        auto s2 = __builtin_amdgcn_permlane16_swap(f2u(z0), f2u(z1), false, false);
        float t = u2f(s2[0]) + u2f(s2[1]);
        t += dpp<XOR2>(t); t += dpp<ROR4>(t); t += dpp<ROR8>(t);
        if (c < 2) __builtin_amdgcn_raw_buffer_store_b16((unsigned short)pk(xr + t, 0.f), mk(out, 4096), ocol, 0, 0);
        if (g == 0 && lane == 0) __builtin_amdgcn_raw_buffer_store_b64(v2u{tag0, tag1}, rws, WS_EPOCH, 0, AUX_SC1);
    }
}

extern "C" void kernel_launch(void* const* d_in, const int* in_sizes, int n_in, void* d_out, int out_size, void* d_ws, size_t ws_size,
                              hipStream_t stream) {
    (void)in_sizes; (void)n_in; (void)out_size; (void)ws_size;
    (void)hipFuncSetAttribute((const void*)fused_decode, hipFuncAttributeMaxDynamicSharedMemorySize, LDS_BYTES);
    hipLaunchKernelGGL(fused_decode, dim3(256), dim3(1024), LDS_BYTES, stream, (const unsigned char*)d_in[0], (const unsigned char*)d_in[1],
                       (const unsigned char*)d_in[2], (const unsigned char*)d_in[3], (const unsigned char*)d_in[4], (const unsigned char*)d_in[6],
                       (const unsigned char*)d_in[7], (const unsigned char*)d_in[8], (unsigned char*)d_out, (unsigned char*)d_ws);
}
```
